```python
import torch

def _softmax(x, axis=-1):
    return torch.softmax(x, dim=axis)
def _gelu(x):
    return torch.nn.functional.gelu(x, approximate="tanh")
def _silu(x):
    return torch.nn.functional.silu(x)
def _sigmoid(x):
    return torch.sigmoid(x)
def _layernorm(x, axis=-1, eps=1e-5):
    m = x.mean(dim=axis, keepdim=True)
    v = x.var(dim=axis, keepdim=True, unbiased=False)
    return (x - m) / torch.sqrt(v + eps)
def _scatter_add(tgt, idx, src, axis=0):
    return tgt.clone().index_add_(axis, idx, src)
def _segment_sum(src, seg, n):
    out = torch.zeros((n,) + src.shape[1:], dtype=src.dtype)
    return out.index_add_(0, seg, src)
def _pad(x, pw):
    flat = tuple(v for pair in reversed(pw) for v in pair)
    return torch.nn.functional.pad(x, flat)

kernel_name = 'interleave_layers__spectral_gfnet__spectral_gfnet__prompt'

def setup_inputs():

    _w = (lambda a: (lambda t: t.to(torch.bfloat16) if t.is_floating_point() else t)(torch.as_tensor(a)))
    def _A_setup():
        B, S, D = 1, 2048, 1024
        Df = D // 2 + 1
        return {
            "x": _w((lambda *s: torch.randn(*s, dtype=torch.bfloat16) * 0.05)(B, S, D)),
            "w_r": _w((lambda *s: torch.randn(*s, dtype=torch.bfloat16) * 0.2)(S, Df) * 0.02),
            "w_i": _w((lambda *s: torch.randn(*s, dtype=torch.bfloat16) * 0.2)(S, Df) * 0.02),
        }
    _A_in = _A_setup()

    def _B_setup():
        B, S, D = 1, 2048, 1024
        Df = D // 2 + 1
        return {
            "x": _w((lambda *s: torch.randn(*s, dtype=torch.bfloat16) * 0.05)(B, S, D)),
            "w_r": _w((lambda *s: torch.randn(*s, dtype=torch.bfloat16) * 0.2)(S, Df) * 0.02),
            "w_i": _w((lambda *s: torch.randn(*s, dtype=torch.bfloat16) * 0.2)(S, Df) * 0.02),
        }
    _B_in = _B_setup()

    return {
        "A_x": _A_in["x"],
        "A_w_r": _A_in["w_r"],
        "A_w_i": _A_in["w_i"],
        "B_x": _B_in["x"],
        "B_w_r": _B_in["w_r"],
        "B_w_i": _B_in["w_i"],
    }

def reference(A_x, A_w_r, A_w_i, B_x, B_w_r, B_w_i):
    def _A_ref(x, w_r, w_i):
        B, S, D = x.shape
        X = (lambda x, axes=(-2,-1): torch.fft.rfft2(x.float(), dim=axes))(x, axes=(-2, -1))
        W = w_r + 1j * w_i
        X = X * W
        y = (lambda x, s, axes=(-2,-1): torch.fft.irfft2(x, s=s, dim=axes).to(torch.bfloat16))(X, (S, D), axes=(-2, -1))
        return y

    def _B_ref(x, w_r, w_i):
        B, S, D = x.shape
        X = (lambda x, axes=(-2,-1): torch.fft.rfft2(x.float(), dim=axes))(x, axes=(-2, -1))
        W = w_r + 1j * w_i
        X = X * W
        y = (lambda x, s, axes=(-2,-1): torch.fft.irfft2(x, s=s, dim=axes).to(torch.bfloat16))(X, (S, D), axes=(-2, -1))
        return y

    x = A_x
    x = x + _A_ref(_layernorm(x, axis=-1), A_w_r, A_w_i)
    x = x + _B_ref(_layernorm(x, axis=-1), B_w_r, B_w_i)
    x = x + _A_ref(_layernorm(x, axis=-1), A_w_r, A_w_i)
    x = x + _B_ref(_layernorm(x, axis=-1), B_w_r, B_w_i)
    return x
```

```cpp
#include <hip/hip_runtime.h>
#include <hip/hip_bf16.h>

typedef unsigned short u16;
typedef unsigned v4u __attribute__((ext_vector_type(4)));
typedef unsigned v2u __attribute__((ext_vector_type(2)));
typedef float v4f __attribute__((ext_vector_type(4)));
typedef float f2 __attribute__((ext_vector_type(2)));
typedef float f16v __attribute__((ext_vector_type(16)));
typedef _Float16 h8 __attribute__((ext_vector_type(8)));
typedef _Float16 h4 __attribute__((ext_vector_type(4)));
typedef _Float16 h2 __attribute__((ext_vector_type(2)));
typedef short s4 __attribute__((ext_vector_type(4)));
#define LAS __attribute__((address_space(3)))
#define DI __device__ __forceinline__
#define MFMA(a, b, c) __builtin_amdgcn_mfma_f32_32x32x16_f16((a), (b), (c), 0, 0, 0)

constexpr unsigned SENT = 0xFFFFFFFFu;
constexpr int SC1 = 16;
constexpr unsigned SPINCAP = 1u << 14;
constexpr float EPS = 1e-5f;

constexpr unsigned XSTR = 16384 + 256;
constexpr unsigned BOX = 256 * XSTR;
constexpr unsigned OFF_XH = 0;
constexpr unsigned OFF_WA = OFF_XH + BOX;
constexpr unsigned OFF_WB = OFF_WA + BOX;
constexpr unsigned OFF_DH = OFF_WB + BOX;
constexpr unsigned LSTR = 32768 + 256;
constexpr unsigned PSZ = 32 * LSTR;
constexpr unsigned OFF_P = OFF_DH + BOX;
constexpr int REP = 8;
constexpr unsigned RSZ = REP * 8192;
constexpr unsigned OFF_R = OFF_P + 3 * PSZ;
constexpr unsigned OFF_SIG = OFF_R + 3 * RSZ;
constexpr unsigned OFF_GW = OFF_SIG + 8192;
constexpr unsigned OFF_TMO = OFF_GW + 1024;
constexpr unsigned WS_USED = OFF_TMO + 256;

constexpr int L_IMG = 0;
constexpr int L_STG = 16384;
constexpr int L_INB = L_STG + 17408;
constexpr int L_IN2 = L_INB + 16384;
constexpr int L_EN = L_IN2 + 16384;
constexpr int L_RS = L_EN + 8192;
constexpr int L_RED = L_RS + 8192;
constexpr int L_MISC = L_RED + 2048;
constexpr int LDS_BYTES = L_MISC + 256;

DI int crow(int reg, int h) { return (reg & 3) + 8 * (reg >> 2) + 4 * h; }
DI int krow(int st, int h, int j) { return 16 * st + 8 * (j >> 2) + 4 * h + (j & 3); }
DI float bf_lo(unsigned v) { return __builtin_bit_cast(float, v << 16); }
DI float bf_hi(unsigned v) { return __builtin_bit_cast(float, v & 0xFFFF0000u); }
DI unsigned pkh(float a, float b) { f2 t = {a, b}; return __builtin_bit_cast(unsigned, __builtin_convertvector(t, h2)); }
DI unsigned pkbf(float a, float b) { unsigned r; asm("v_cvt_pk_bf16_f32 %0, %1, %2" : "=v"(r) : "v"(a), "v"(b)); return r; }
DI unsigned nosent(unsigned v) { return v < 0xFFFFFFFEu ? v : 0xFFFFFFFEu; }
DI v4u nosent4(v4u v) { return v4u{nosent(v.x), nosent(v.y), nosent(v.z), nosent(v.w)}; }
DI bool has_sent(v4u v) { return (v.x == SENT) | (v.y == SENT) | (v.z == SENT) | (v.w == SENT); }
DI float rsin(float rev) { return __builtin_amdgcn_sinf(rev); }
DI float rcos(float rev) { return __builtin_amdgcn_cosf(rev); }
template <int CTRL, int RM> DI float dpp_add(float v) {
  int o = __builtin_amdgcn_update_dpp(0, __builtin_bit_cast(int, v), CTRL, RM, 0xf, true);
  return v + __builtin_bit_cast(float, o);
}
DI float wave_total(float v) {
  v = dpp_add<0x111, 0xf>(v); v = dpp_add<0x112, 0xf>(v); v = dpp_add<0x114, 0xf>(v); v = dpp_add<0x118, 0xf>(v);
  v = dpp_add<0x142, 0xa>(v); v = dpp_add<0x143, 0xc>(v);
  return __builtin_bit_cast(float, __builtin_amdgcn_readlane(__builtin_bit_cast(int, v), 63));
}
template <int CTRL, int RM> DI unsigned dpp_maxu(unsigned v) {
  unsigned o = (unsigned)__builtin_amdgcn_update_dpp(0, (int)v, CTRL, RM, 0xf, true);
  return v > o ? v : o;
}
DI unsigned wave_maxu(unsigned v) {
  v = dpp_maxu<0x111, 0xf>(v); v = dpp_maxu<0x112, 0xf>(v); v = dpp_maxu<0x114, 0xf>(v); v = dpp_maxu<0x118, 0xf>(v);
  v = dpp_maxu<0x142, 0xa>(v); v = dpp_maxu<0x143, 0xc>(v);
  return (unsigned)__builtin_amdgcn_readlane((int)v, 63);
}
DI h8 frag(const f16v& x, int s) {
  v4u p = {pkh(x[8 * s], x[8 * s + 1]), pkh(x[8 * s + 2], x[8 * s + 3]), pkh(x[8 * s + 4], x[8 * s + 5]), pkh(x[8 * s + 6], x[8 * s + 7])};
  return __builtin_bit_cast(h8, p);
}
DI h8 neg8(h8 v) { v4u p = __builtin_bit_cast(v4u, v); p ^= 0x80008000u; return __builtin_bit_cast(h8, p); }
DI h4 tr_read(char* smem, int off) { return __builtin_bit_cast(h4, __builtin_amdgcn_ds_read_tr16_b64_v4i16((LAS s4*)(smem + off))); }
DI h8 cat(h4 a, h4 b) { return __builtin_shufflevector(a, b, 0, 1, 2, 3, 4, 5, 6, 7); }
DI f16v zero16() { f16v z; for (int i = 0; i < 16; ++i) z[i] = 0.f; return z; }

typedef __amdgpu_buffer_rsrc_t rsrc_t;
DI v4u ld_sc(rsrc_t rs, unsigned off) { return __builtin_amdgcn_raw_buffer_load_b128(rs, off, 0, SC1); }
DI void st_sc(rsrc_t rs, unsigned off, v4u v) { __builtin_amdgcn_raw_buffer_store_b128(v, rs, off, 0, SC1); }
template <int N> DI void poll(rsrc_t rs, unsigned off, unsigned stride, v4u (&x)[N]) {
  for (unsigned spins = 0;; ++spins) {
    asm volatile("" ::: "memory");
    bool bad = false;
#pragma unroll
    for (int m = 0; m < N; ++m) x[m] = ld_sc(rs, off + m * stride);
#pragma unroll
    for (int m = 0; m < N; ++m) bad |= has_sent(x[m]);
    if (!__any(bad)) break;
    if (spins > SPINCAP) { __builtin_amdgcn_raw_buffer_store_b32(1u, rs, OFF_TMO, 0, SC1); break; }
    __builtin_amdgcn_s_sleep(1);
  }
}
DI void inbox_to_lds(rsrc_t rs, unsigned gbase, char* dst, int tid) {
  v4u x[4];
  poll<4>(rs, gbase + tid * 16, 4096, x);
#pragma unroll
  for (int m = 0; m < 4; ++m) {
    const int p = m * 256 + tid, i = p >> 2;
    *(v4u*)(dst + i * 64 + (((p & 3) ^ ((i >> 2) & 3)) << 4)) = x[m];
  }
}
DI v4u rec_read(const char* src, int i, int sub) { return *(const v4u*)(src + i * 64 + ((sub ^ ((i >> 2) & 3)) << 4)); }

template <int PH>
__global__ __launch_bounds__(256, 1) void gf_kernel(const u16* __restrict__ x, const u16* __restrict__ awr, const u16* __restrict__ awi,
                                                    const u16* __restrict__ bwr, const u16* __restrict__ bwi, u16* __restrict__ out,
                                                    unsigned char* __restrict__ ws) {
  extern __shared__ __attribute__((aligned(16))) char smem[];
  const int tid = threadIdx.x, lane = tid & 63, w = __builtin_amdgcn_readfirstlane(tid >> 6);
  const int l31 = lane & 31, h = lane >> 5, b = blockIdx.x;
  const rsrc_t rs = __builtin_amdgcn_make_buffer_rsrc(ws, 0, WS_USED, 0x00020000);

  v4u xr[2][2];
  if (PH & 5) {
#pragma unroll
    for (int a = 0; a < 2; ++a) {
      const int s = 32 * krow(b >> 6, (b >> 5) & 1, 2 * w + a) + (b & 31);
      const v4u* p = (const v4u*)(x + (size_t)s * 1024 + l31 * 32 + h * 16);
      xr[a][0] = p[0]; xr[a][1] = p[1];
    }
  }

  if (PH & 1) {
    v4u wv[2][2][2];
#pragma unroll
    for (int a = 0; a < 2; ++a) {
      const int f = 32 * (b >> 7) + (b & 31) + 64 * krow((b >> 6) & 1, (b >> 5) & 1, 2 * w + a);
      const size_t e = (size_t)f * 513 + 1 + 8 * lane;
      __builtin_memcpy(&wv[0][a][0], awr + e, 16); __builtin_memcpy(&wv[0][a][1], awi + e, 16);
      __builtin_memcpy(&wv[1][a][0], bwr + e, 16); __builtin_memcpy(&wv[1][a][1], bwi + e, 16);
    }
    {
      const v4u sv = {SENT, SENT, SENT, SENT};
      for (unsigned o = OFF_P + (b * 256 + tid) * 16; o < OFF_TMO; o += 65536 * 16) if (o < OFF_SIG || o >= OFF_GW) st_sc(rs, o, sv);
      if (b == 0 && tid == 0) __builtin_amdgcn_raw_buffer_store_b32(0u, rs, OFF_TMO, 0, SC1);
    }
    h8 c32[2], ns32[2], a1[2], a2[2];
    float twc[16], tws[16];
#pragma unroll
    for (int st = 0; st < 2; ++st)
#pragma unroll
      for (int j = 0; j < 8; ++j) {
        const int kr = krow(st, h, j);
        const float an = (float)((l31 * kr) & 31) * (1.f / 32.f);
        c32[st][j] = (_Float16)rcos(an); ns32[st][j] = (_Float16)(-rsin(an));
        const float bn = (float)(((l31 & 15) * kr + (l31 >> 4) * 8) & 31) * (1.f / 32.f);
        a1[st][j] = (_Float16)rcos(bn); a2[st][j] = (_Float16)rsin(bn);
      }
#pragma unroll
    for (int r = 0; r < 16; ++r) {
      const float an = (float)(crow(r, h) * l31) * (1.f / 1024.f);
      twc[r] = rcos(an) * (1.f / 32.f); tws[r] = rsin(an) * (1.f / 32.f);
    }
    float nyq[2], sig[2];
#pragma unroll
    for (int a = 0; a < 2; ++a) {
      float v[16];
#pragma unroll
      for (int k = 0; k < 8; ++k) { const unsigned d = xr[a][k >> 2][k & 3]; v[2 * k] = bf_lo(d); v[2 * k + 1] = bf_hi(d); }
      float se = 0.f, so = 0.f;
#pragma unroll
      for (int k = 0; k < 8; ++k) { se += v[2 * k]; so += v[2 * k + 1]; }
      se = wave_total(se); so = wave_total(so);
      const float m = (se + so) * (1.f / 1024.f);
      float q2 = 0.f;
#pragma unroll
      for (int k = 0; k < 16; ++k) { v[k] -= m; q2 += v[k] * v[k]; }
      q2 = wave_total(q2) * (1.f / 1024.f) + EPS;
      const float r0 = __builtin_amdgcn_rsqf(q2);
      sig[a] = q2 * r0;
      nyq[a] = (se - so) * r0 * (1.f / 32.f);
      v4u g0, g1;
#pragma unroll
      for (int k = 0; k < 4; ++k) { g0[k] = pkh(v[2 * k] * r0, v[2 * k + 1] * r0); g1[k] = pkh(v[8 + 2 * k] * r0, v[9 + 2 * k] * r0); }
      char* img = smem + L_IMG + (2 * w + a) * 2048 + l31 * 64 + h * 32;
      *(v4u*)img = g0; *(v4u*)(img + 16) = g1;
    }
    if (lane == 0) {
      v2u sv = {nosent(__builtin_bit_cast(unsigned, sig[0])), nosent(__builtin_bit_cast(unsigned, sig[1]))};
      __builtin_amdgcn_raw_buffer_store_b64(sv, rs, OFF_SIG + b * 32 + w * 8, 0, SC1);
    }
    f16v o[2];
#pragma unroll
    for (int a = 0; a < 2; ++a) {
      const int g = lane >> 4, i16 = lane & 15;
      const int tb = L_IMG + (2 * w + a) * 2048 + (4 * h + (i16 >> 2)) * 64 + (16 * (g & 1) + 4 * (i16 & 3)) * 2;
      f16v dr = zero16(), di = zero16();
#pragma unroll
      for (int st = 0; st < 2; ++st) {
        const h8 A = cat(tr_read(smem, tb + st * 1024), tr_read(smem, tb + st * 1024 + 512));
        dr = MFMA(A, c32[st], dr); di = MFMA(A, ns32[st], di);
      }
      f16v er, ei;
#pragma unroll
      for (int r = 0; r < 16; ++r) { er[r] = dr[r] * twc[r] + di[r] * tws[r]; ei[r] = di[r] * twc[r] - dr[r] * tws[r]; }
      f16v d2 = zero16();
#pragma unroll
      for (int st = 0; st < 2; ++st) { d2 = MFMA(a1[st], frag(er, st), d2); d2 = MFMA(a2[st], frag(ei, st), d2); }
      if (lane == 0) { d2[0] = nyq[a]; d2[8] = 0.f; }
      o[a] = d2;
    }
    {
      unsigned* stg = (unsigned*)(smem + L_STG) + w * 1088 + l31 + 128 * h;
#pragma unroll
      for (int r = 0; r < 8; ++r) {
        const int k2o = 32 * ((r & 3) + 8 * (r >> 2));
        stg[k2o] = pkh(o[0][r], o[1][r]);
        stg[k2o + 528] = pkh(o[0][r + 8], o[1][r + 8]);
      }
    }
    __syncthreads();
    {
      const unsigned* stg = (const unsigned*)(smem + L_STG);
#pragma unroll
      for (int m = 0; m < 4; ++m) {
        const int p = m * 256 + tid, kap = p >> 1, ci = p & 1;
        const unsigned* sp = stg + ci * 528 + kap;
        v4u v = {sp[0], sp[1088], sp[2176], sp[3264]};
        st_sc(rs, OFF_XH + (p >> 2) * XSTR + b * 64 + (p & 3) * 16, nosent4(v));
      }
    }
#pragma unroll
    for (int mt = 0; mt < 2; ++mt) {
      __syncthreads();
      unsigned* stg = (unsigned*)(smem + L_STG);
#pragma unroll
      for (int a = 0; a < 2; ++a) {
        v4u lo, hi;
#pragma unroll
        for (int k = 0; k < 2; ++k) {
          lo[2 * k] = __builtin_amdgcn_perm(wv[mt][a][1][k], wv[mt][a][0][k], 0x05040100u);
          lo[2 * k + 1] = __builtin_amdgcn_perm(wv[mt][a][1][k], wv[mt][a][0][k], 0x07060302u);
          hi[2 * k] = __builtin_amdgcn_perm(wv[mt][a][1][k + 2], wv[mt][a][0][k + 2], 0x05040100u);
          hi[2 * k + 1] = __builtin_amdgcn_perm(wv[mt][a][1][k + 2], wv[mt][a][0][k + 2], 0x07060302u);
        }
        *(v4u*)(stg + (2 * w + a) * 540 + lane * 8) = lo;
        *(v4u*)(stg + (2 * w + a) * 540 + lane * 8 + 4) = hi;
      }
      __syncthreads();
#pragma unroll
      for (int m = 0; m < 4; ++m) {
        const int p = m * 256 + tid, kap = p >> 1, qh = p & 1;
        const unsigned* sp = stg + qh * 4 * 540 + ((kap - 1) & 511);
        v4u v = {sp[0], sp[540], sp[1080], sp[1620]};
        st_sc(rs, (mt ? OFF_WB : OFF_WA) + (p >> 2) * XSTR + b * 64 + (p & 3) * 16, nosent4(v));
      }
    }
    __syncthreads();
  }

  float gscale = 1.f;
  if (PH & 2) {
    const int c = w >> 1, hf = w & 1;
    const bool nyqcol = (b == 0) && (c == 0);
    h8 C64[4], S64[4], N64[4], CA[2], SA[2], NA[2], CB[2], SB[2], NB[2];
    float tc[16], ts[16];
#pragma unroll
    for (int t = 0; t < 4; ++t) {
#pragma unroll
      for (int j = 0; j < 8; ++j) {
        const float an = (float)(((32 * hf + l31) * krow(t, h, j)) & 63) * (1.f / 64.f);
        C64[t][j] = (_Float16)rcos(an); S64[t][j] = (_Float16)rsin(an);
      }
      N64[t] = neg8(S64[t]);
    }
#pragma unroll
    for (int st = 0; st < 2; ++st) {
#pragma unroll
      for (int j = 0; j < 8; ++j) {
        const int kr = krow(st, h, j);
        const float an = (float)((kr * (2 * l31 + hf)) & 63) * (1.f / 64.f);
        CA[st][j] = (_Float16)rcos(an); SA[st][j] = (_Float16)rsin(an);
        const float bn = (float)((l31 * (2 * kr + hf)) & 63) * (1.f / 64.f);
        CB[st][j] = (_Float16)rcos(bn); SB[st][j] = (_Float16)rsin(bn);
      }
      NA[st] = neg8(SA[st]); NB[st] = neg8(SB[st]);
    }
#pragma unroll
    for (int r = 0; r < 16; ++r) {
      const float an = (float)(crow(r, h) * l31) * (1.f / 2048.f);
      tc[r] = rcos(an) * 0.125f; ts[r] = rsin(an) * 0.125f;
    }
    inbox_to_lds(rs, OFF_XH + b * XSTR, smem + L_INB, tid);
    unsigned wp[2][16];
    float wun[2];
    unsigned emax = 0;
#pragma unroll
    for (int mt = 0; mt < 2; ++mt) {
      inbox_to_lds(rs, (mt ? OFF_WB : OFF_WA) + b * XSTR, smem + L_IN2, tid);
      __syncthreads();
      unsigned mx = 0;
#pragma unroll
      for (int tt = 0; tt < 2; ++tt)
#pragma unroll
        for (int k = 0; k < 2; ++k) {
          const v4u v = rec_read(smem + L_IN2, 64 * (2 * hf + tt) + lane, 2 * c + k);
#pragma unroll
          for (int e = 0; e < 4; ++e) {
            wp[mt][8 * tt + 4 * k + e] = v[e];
            const unsigned a0 = v[e] & 0x7FFFu, a1 = (v[e] >> 16) & 0x7FFFu;
            mx = mx > a0 ? mx : a0; mx = mx > a1 ? mx : a1;
          }
        }
      mx = wave_maxu(mx);
      if (lane == 0) *(unsigned*)(smem + L_MISC + (mt * 4 + w) * 4) = mx;
      __syncthreads();
      mx = *(unsigned*)(smem + L_MISC + (mt * 4 + (w ^ 1)) * 4) > mx ? *(unsigned*)(smem + L_MISC + (mt * 4 + (w ^ 1)) * 4) : mx;
      unsigned E = mx >> 7; E = E < 4u ? 4u : (E > 248u ? 248u : E);
      const float sc = __builtin_bit_cast(float, (251u - E) << 23);
      wun[mt] = __builtin_bit_cast(float, (E - 2u) << 23);
#pragma unroll
      for (int r = 0; r < 16; ++r) wp[mt][r] = pkbf(bf_lo(wp[mt][r]) * sc, bf_hi(wp[mt][r]) * sc);
      const v4u am = *(const v4u*)(smem + L_MISC + mt * 16);
      unsigned t0 = am.x > am.y ? am.x : am.y, t1 = am.z > am.w ? am.z : am.w; t0 = t0 > t1 ? t0 : t1;
      emax = emax > t0 ? emax : t0;
    }
    if (tid == 0) __builtin_amdgcn_raw_buffer_store_b32(emax, rs, OFF_GW + b * 4, 0, SC1);
    {
      v4u y[2];
      poll<2>(rs, OFF_SIG + w * 2048 + lane * 16, 1024, y);
      *(v4u*)(smem + L_RS + w * 2048 + lane * 16) = y[0]; *(v4u*)(smem + L_RS + w * 2048 + 1024 + lane * 16) = y[1];
    }
    __syncthreads();
    f16v zr, zi;
    float sg0[16];
#pragma unroll
    for (int tt = 0; tt < 2; ++tt) {
      const int i = 64 * (2 * hf + tt) + lane;
      const h8 ur = __builtin_bit_cast(h8, rec_read(smem + L_INB, i, 2 * c)), ui = __builtin_bit_cast(h8, rec_read(smem + L_INB, i, 2 * c + 1));
      const v4f s0 = *(const v4f*)(smem + L_RS + i * 32), s1 = *(const v4f*)(smem + L_RS + i * 32 + 16);
#pragma unroll
      for (int j = 0; j < 8; ++j) {
        const float sg = j < 4 ? s0[j & 3] : s1[j & 3];
        sg0[8 * tt + j] = sg;
        zr[8 * tt + j] = (float)ur[j] * sg; zi[8 * tt + j] = (float)ui[j] * sg;
      }
    }
    const float ew = nyqcol ? (1.f / 1024.f) : (2.f / 1024.f);
    const float imk = nyqcol ? 0.f : 1.f;

#pragma unroll
    for (int layer = 0; layer < 4; ++layer) {
      h8 Ur[4], Ui[4];
      if (layer == 0) {
#pragma unroll
        for (int t = 0; t < 4; ++t) {
          Ur[t] = __builtin_bit_cast(h8, rec_read(smem + L_INB, 64 * t + lane, 2 * c));
          Ui[t] = __builtin_bit_cast(h8, rec_read(smem + L_INB, 64 * t + lane, 2 * c + 1));
        }
      } else {
        f16v ur, ui;
#pragma unroll
        for (int tt = 0; tt < 2; ++tt) {
          const int i = 64 * (2 * hf + tt) + lane;
          const v4f s0 = *(const v4f*)(smem + L_RS + i * 32), s1 = *(const v4f*)(smem + L_RS + i * 32 + 16);
#pragma unroll
          for (int j = 0; j < 8; ++j) { const float rr = j < 4 ? s0[j & 3] : s1[j & 3]; ur[8 * tt + j] = zr[8 * tt + j] * rr; ui[8 * tt + j] = zi[8 * tt + j] * rr; }
        }
        char* ex = smem + L_IMG + c * 8192 + lane * 16;
        h8 o0 = frag(ur, 0), o1 = frag(ui, 0), o2 = frag(ur, 1), o3 = frag(ui, 1);
        *(h8*)(ex + (hf * 4 + 0) * 1024) = o0; *(h8*)(ex + (hf * 4 + 1) * 1024) = o1;
        *(h8*)(ex + (hf * 4 + 2) * 1024) = o2; *(h8*)(ex + (hf * 4 + 3) * 1024) = o3;
        __syncthreads();
#pragma unroll
        for (int t = 0; t < 4; ++t) { Ur[t] = *(const h8*)(ex + (t * 2) * 1024); Ui[t] = *(const h8*)(ex + (t * 2 + 1) * 1024); }
      }
      f16v ar = zero16(), ai = zero16();
#pragma unroll
      for (int t = 0; t < 4; ++t) {
        ar = MFMA(Ur[t], C64[t], ar); ai = MFMA(Ui[t], C64[t], ai);
        ar = MFMA(Ui[t], S64[t], ar); ai = MFMA(Ur[t], N64[t], ai);
      }
      f16v br, bi;
#pragma unroll
      for (int r = 0; r < 16; ++r) { br[r] = ar[r] * tc[r] + ai[r] * ts[r]; bi[r] = ai[r] * tc[r] - ar[r] * ts[r]; }
      ar = zero16(); ai = zero16();
#pragma unroll
      for (int st = 0; st < 2; ++st) {
        const h8 fr = frag(br, st), fi = frag(bi, st);
        ar = MFMA(CA[st], fr, ar); ai = MFMA(CA[st], fi, ai);
        ar = MFMA(SA[st], fi, ar); ai = MFMA(NA[st], fr, ai);
      }
#pragma unroll
      for (int r = 0; r < 16; ++r) {
        const float wr = bf_lo(wp[layer & 1][r]), wi = bf_hi(wp[layer & 1][r]);
        br[r] = ar[r] * wr - ai[r] * wi; bi[r] = ar[r] * wi + ai[r] * wr;
      }
      ar = zero16(); ai = zero16();
#pragma unroll
      for (int st = 0; st < 2; ++st) {
        const h8 fr = frag(br, st), fi = frag(bi, st);
        ar = MFMA(fr, CB[st], ar); ai = MFMA(fi, CB[st], ai);
        ar = MFMA(fi, NB[st], ar); ai = MFMA(fr, SB[st], ai);
      }
#pragma unroll
      for (int r = 0; r < 16; ++r) { br[r] = ar[r] * tc[r] - ai[r] * ts[r]; bi[r] = ai[r] * tc[r] + ar[r] * ts[r]; }
      {
        char* ex = smem + L_IN2 + c * 8192 + lane * 16;
        *(h8*)(ex + (hf * 4 + 0) * 1024) = frag(br, 0); *(h8*)(ex + (hf * 4 + 1) * 1024) = frag(bi, 0);
        *(h8*)(ex + (hf * 4 + 2) * 1024) = frag(br, 1); *(h8*)(ex + (hf * 4 + 3) * 1024) = frag(bi, 1);
        __syncthreads();
#pragma unroll
        for (int t = 0; t < 4; ++t) { Ur[t] = *(const h8*)(ex + (t * 2) * 1024); Ui[t] = *(const h8*)(ex + (t * 2 + 1) * 1024); }
      }
      ar = zero16(); ai = zero16();
#pragma unroll
      for (int t = 0; t < 4; ++t) {
        ar = MFMA(C64[t], Ur[t], ar); ai = MFMA(C64[t], Ui[t], ai);
        ar = MFMA(N64[t], Ui[t], ar); ai = MFMA(S64[t], Ur[t], ai);
      }
      const float ur_s = wun[layer & 1], ui_s = ur_s * imk;
#pragma unroll
      for (int r = 0; r < 16; ++r) { zr[r] += ar[r] * ur_s; zi[r] += ai[r] * ui_s; }

      if (layer < 3) {
        float e[16];
#pragma unroll
        for (int r = 0; r < 16; ++r) e[r] = (zr[r] * zr[r] + zi[r] * zi[r]) * ew;
        float* en = (float*)(smem + L_EN);
        const int go = c ? 0 : 8, ko = c ? 8 : 0;
        v4f g0, g1, m0, m1;
        if (c == 0) { g0 = v4f{e[8], e[9], e[10], e[11]}; g1 = v4f{e[12], e[13], e[14], e[15]}; m0 = v4f{e[0], e[1], e[2], e[3]}; m1 = v4f{e[4], e[5], e[6], e[7]}; }
        else        { g0 = v4f{e[0], e[1], e[2], e[3]}; g1 = v4f{e[4], e[5], e[6], e[7]}; m0 = v4f{e[8], e[9], e[10], e[11]}; m1 = v4f{e[12], e[13], e[14], e[15]}; }
        (void)go; (void)ko;
        *(v4f*)(en + (w * 2) * 256 + lane * 4) = g0; *(v4f*)(en + (w * 2 + 1) * 256 + lane * 4) = g1;
        __syncthreads();
        m0 += *(const v4f*)(en + ((w ^ 2) * 2) * 256 + lane * 4); m1 += *(const v4f*)(en + ((w ^ 2) * 2 + 1) * 256 + lane * 4);
        v4u pk;
#pragma unroll
        for (int k = 0; k < 2; ++k) {
          pk[k] = ((__builtin_bit_cast(unsigned, m0[2 * k]) + 0x4000u) >> 15) | (((__builtin_bit_cast(unsigned, m0[2 * k + 1]) + 0x4000u) >> 15) << 16);
          pk[k + 2] = ((__builtin_bit_cast(unsigned, m1[2 * k]) + 0x4000u) >> 15) | (((__builtin_bit_cast(unsigned, m1[2 * k + 1]) + 0x4000u) >> 15) << 16);
        }
        const unsigned pb = OFF_P + layer * PSZ, rb = OFF_R + layer * RSZ;
        st_sc(rs, pb + ((2 * hf + c) * 8 + (lane >> 3)) * LSTR + b * 128 + (lane & 7) * 16, nosent4(pk));
        if (b < 64) {
          asm volatile("s_waitcnt vmcnt(0)" ::: "memory");
          v4u xx[4];
          poll<4>(rs, pb + (b >> 1) * LSTR + (tid >> 2) * 128 + (b & 1) * 64 + (tid & 3) * 16, 64 * 128, xx);
          float s[8] = {0, 0, 0, 0, 0, 0, 0, 0};
#pragma unroll
          for (int m = 0; m < 4; ++m)
#pragma unroll
            for (int k = 0; k < 4; ++k) {
              s[2 * k] += __builtin_bit_cast(float, (xx[m][k] & 0xFFFFu) << 15);
              s[2 * k + 1] += __builtin_bit_cast(float, (xx[m][k] >> 16) << 15);
            }
#pragma unroll
          for (int k = 0; k < 8; ++k) {
            float v = s[k];
            v = dpp_add<0x124, 0xf>(v); v = dpp_add<0x128, 0xf>(v);
            v += __shfl_xor(v, 16); v += __shfl_xor(v, 32);
            s[k] = v;
          }
          float* red = (float*)(smem + L_RED);
          if (lane < 4) { *(v4f*)(red + w * 32 + lane * 8) = v4f{s[0], s[1], s[2], s[3]}; *(v4f*)(red + w * 32 + lane * 8 + 4) = v4f{s[4], s[5], s[6], s[7]}; }
          __syncthreads();
          if (w == 0) {
            const int u = lane & 7;
            v4f q = *(const v4f*)(red + u * 4) + *(const v4f*)(red + 32 + u * 4) + *(const v4f*)(red + 64 + u * 4) + *(const v4f*)(red + 96 + u * 4);
#pragma unroll
            for (int k = 0; k < 4; ++k) q[k] = __builtin_amdgcn_rsqf(q[k] + EPS);
            st_sc(rs, rb + (lane >> 3) * 8192 + b * 128 + u * 16, nosent4(__builtin_bit_cast(v4u, q)));
          }
        }
        {
          v4u y[2];
          poll<2>(rs, rb + (b & 7) * 8192 + w * 2048 + lane * 16, 1024, y);
          *(v4u*)(smem + L_RS + w * 2048 + lane * 16) = y[0]; *(v4u*)(smem + L_RS + w * 2048 + 1024 + lane * 16) = y[1];
        }
        __syncthreads();
      }
    }
    {
      for (unsigned spins = 0;; ++spins) {
        asm volatile("" ::: "memory");
        const unsigned g = __builtin_amdgcn_raw_buffer_load_b32(rs, OFF_GW + tid * 4, 0, SC1);
        if (!__any(g == SENT) || spins > SPINCAP) {
          const unsigned mx = wave_maxu(g);
          if (lane == 0) *(unsigned*)(smem + L_MISC + 64 + w * 4) = mx;
          break;
        }
        __builtin_amdgcn_s_sleep(1);
      }
      __syncthreads();
      const v4u am = *(const v4u*)(smem + L_MISC + 64);
      unsigned t0 = am.x > am.y ? am.x : am.y, t1 = am.z > am.w ? am.z : am.w; t0 = t0 > t1 ? t0 : t1;
      unsigned E = t0 >> 7; E = E < 4u ? 4u : (E > 248u ? 248u : E);
      gscale = __builtin_bit_cast(float, (E + 1u) << 23);
      const float ginv = __builtin_bit_cast(float, (253u - E) << 23);
      const int key = (b >> 4) & 3;
#pragma unroll
      for (int tt = 0; tt < 2; ++tt) {
        const int i = 64 * (2 * hf + tt) + lane;
        const h8 ur = __builtin_bit_cast(h8, rec_read(smem + L_INB, i, 2 * c)), ui = __builtin_bit_cast(h8, rec_read(smem + L_INB, i, 2 * c + 1));
#pragma unroll
        for (int qp = 0; qp < 4; ++qp) {
          const int r = 8 * tt + 2 * qp;
          const float d0 = (zr[r] - (float)ur[2 * qp] * sg0[r]) * ginv, d1 = (zr[r + 1] - (float)ur[2 * qp + 1] * sg0[r + 1]) * ginv;
          const float e0 = (zi[r] - (float)ui[2 * qp] * sg0[r]) * ginv, e1 = (zi[r + 1] - (float)ui[2 * qp + 1] * sg0[r + 1]) * ginv;
          const int slot = qp ^ key;
          const int sub = (2 * c + (slot >> 1)) ^ ((i >> 2) & 3);
          *(v2u*)(smem + L_STG + i * 64 + sub * 16 + (slot & 1) * 8) = v2u{pkh(d0, d1), pkh(e0, e1)};
        }
      }
      __syncthreads();
#pragma unroll
      for (int m = 0; m < 4; ++m) {
        const int p = m * 256 + tid;
        st_sc(rs, OFF_DH + (p >> 2) * XSTR + b * 64 + (p & 3) * 16, nosent4(rec_read(smem + L_STG, p >> 2, p & 3)));
      }
    }
  }

  if (PH & 4) {
    if (!(PH & 2)) {
      const unsigned g = __builtin_amdgcn_raw_buffer_load_b32(rs, OFF_GW + tid * 4, 0, SC1);
      const unsigned mx = wave_maxu(g);
      if (lane == 0) *(unsigned*)(smem + L_MISC + 64 + w * 4) = mx;
      __syncthreads();
      const v4u am = *(const v4u*)(smem + L_MISC + 64);
      unsigned t0 = am.x > am.y ? am.x : am.y, t1 = am.z > am.w ? am.z : am.w; t0 = t0 > t1 ? t0 : t1;
      unsigned E = t0 >> 7; E = E < 4u ? 4u : (E > 248u ? 248u : E);
      gscale = __builtin_bit_cast(float, (E + 1u) << 23);
    }
    const int n2 = 16 * ((l31 >> 2) & 1) + 4 * (l31 >> 3) + (l31 & 3);
    h8 Cq, Sq, Cz[2], Nz[2];
    float tc[16], ts[16];
#pragma unroll
    for (int j = 0; j < 8; ++j) {
      const float an = (float)(((8 * h + j) * n2) & 31) * (1.f / 32.f);
      Cq[j] = (_Float16)rcos(an); Sq[j] = (_Float16)rsin(an);
#pragma unroll
      for (int s2 = 0; s2 < 2; ++s2) {
        const int k1 = 8 * (2 * s2 + (j >> 2)) + 2 * (j & 3) + h;
        const float bn = (float)((k1 * l31) & 31) * (1.f / 32.f);
        Cz[s2][j] = (_Float16)rcos(bn); Nz[s2][j] = (_Float16)(-rsin(bn));
      }
    }
#pragma unroll
    for (int r = 0; r < 16; ++r) {
      const float an = (float)((2 * r + h) * n2) * (1.f / 1024.f);
      tc[r] = rcos(an) * 0.25f; ts[r] = rsin(an) * 0.25f;
    }
    {
      v4u y[4];
      poll<4>(rs, OFF_DH + b * XSTR + tid * 16, 4096, y);
      if (tid < 2) { *(v4u*)(smem + L_MISC + 128 + tid * 16) = y[0]; y[0] = v4u{0u, 0u, 0u, 0u}; }
#pragma unroll
      for (int m = 0; m < 4; ++m) *(v4u*)(smem + L_IN2 + (m * 256 + tid) * 16) = y[m];
    }
    __syncthreads();
    f16v er[2], ei[2];
    {
      const int g = lane >> 4, i16 = lane & 15, qq = i16 >> 2, pp = i16 & 3;
      const int tb = L_IN2 + (4 * (g & 1) + pp + 32 * (8 * h + qq)) * 32 + ((w ^ qq) << 3);
#pragma unroll
      for (int T = 0; T < 4; ++T) {
        const h8 A = cat(tr_read(smem, tb + T * 256), tr_read(smem, tb + T * 256 + 4096));
        const f16v pa = MFMA(A, Cq, zero16()), pb = MFMA(A, Sq, zero16());
#pragma unroll
        for (int a = 0; a < 4; ++a)
#pragma unroll
          for (int q0 = 0; q0 < 2; ++q0) {
            const float xr0 = pa[4 * a + q0] - pb[4 * a + 2 + q0], xi0 = pb[4 * a + q0] + pa[4 * a + 2 + q0];
            const int r = 4 * T + a;
            er[q0][r] = xr0 * tc[r] - xi0 * ts[r]; ei[q0][r] = xi0 * tc[r] + xr0 * ts[r];
          }
      }
    }
    const h2 nv = *(const h2*)(smem + L_MISC + 128 + w * 8);
    const float osc = gscale * 0.25f;
#pragma unroll
    for (int a = 0; a < 2; ++a) {
      f16v acc = zero16();
#pragma unroll
      for (int s2 = 0; s2 < 2; ++s2) { acc = MFMA(frag(er[a], s2), Cz[s2], acc); acc = MFMA(frag(ei[a], s2), Nz[s2], acc); }
      const float ny = (float)nv[a] * gscale * (1.f / 32.f);
      v4u ov[2];
#pragma unroll
      for (int k = 0; k < 8; ++k) {
        const unsigned d = xr[a][k >> 2][k & 3];
        ov[k >> 2][k & 3] = pkbf(bf_lo(d) + (acc[2 * k] * osc + ny), bf_hi(d) + (acc[2 * k + 1] * osc - ny));
      }
      const int s = 32 * krow(b >> 6, (b >> 5) & 1, 2 * w + a) + (b & 31);
      v4u* p = (v4u*)(out + (size_t)s * 1024 + l31 * 32 + h * 16);
      p[0] = ov[0]; p[1] = ov[1];
    }
  }
}

extern "C" void kernel_launch(void* const* d_in, const int* in_sizes, int n_in, void* d_out, int out_size, void* d_ws, size_t ws_size,
                              hipStream_t stream) {
  const u16* x = (const u16*)d_in[0];
  const u16* awr = (const u16*)d_in[1];
  const u16* awi = (const u16*)d_in[2];
  const u16* bwr = (const u16*)d_in[4];
  const u16* bwi = (const u16*)d_in[5];
  u16* out = (u16*)d_out;
  unsigned char* ws = (unsigned char*)d_ws;
  (void)hipFuncSetAttribute((const void*)gf_kernel<1>, hipFuncAttributeMaxDynamicSharedMemorySize, LDS_BYTES);
  (void)hipFuncSetAttribute((const void*)gf_kernel<2>, hipFuncAttributeMaxDynamicSharedMemorySize, LDS_BYTES);
  (void)hipFuncSetAttribute((const void*)gf_kernel<4>, hipFuncAttributeMaxDynamicSharedMemorySize, LDS_BYTES);
  gf_kernel<1><<<256, 256, LDS_BYTES, stream>>>(x, awr, awi, bwr, bwi, out, ws);
  gf_kernel<2><<<256, 256, LDS_BYTES, stream>>>(x, awr, awi, bwr, bwi, out, ws);
  gf_kernel<4><<<256, 256, LDS_BYTES, stream>>>(x, awr, awi, bwr, bwi, out, ws);
}
```

```cpp
#include <hip/hip_runtime.h>
#include <hip/hip_bf16.h>

typedef unsigned short u16;
typedef unsigned v4u __attribute__((ext_vector_type(4)));
typedef unsigned v2u __attribute__((ext_vector_type(2)));
typedef float v4f __attribute__((ext_vector_type(4)));
typedef float f2 __attribute__((ext_vector_type(2)));
typedef float f16v __attribute__((ext_vector_type(16)));
typedef _Float16 h8 __attribute__((ext_vector_type(8)));
typedef _Float16 h4 __attribute__((ext_vector_type(4)));
typedef _Float16 h2 __attribute__((ext_vector_type(2)));
typedef __bf16 b8 __attribute__((ext_vector_type(8)));
typedef short s4 __attribute__((ext_vector_type(4)));
#define LAS __attribute__((address_space(3)))
#if defined(__HIP_DEVICE_COMPILE__)
#define NOPK __attribute__((target("no-packed-fp32-ops")))
#else
#define NOPK
#endif
#define DI __device__ __forceinline__ NOPK
#define MFMA(a, b, c) __builtin_amdgcn_mfma_f32_32x32x16_f16((a), (b), (c), 0, 0, 0)

constexpr unsigned SENT = 0xFFFFFFFFu;
constexpr int SC1 = 16;
constexpr unsigned SPINCAP = 1u << 17;
constexpr float EPS = 1e-5f;
constexpr int NLAYER = 4;
constexpr unsigned OOB = 0xFFFFFF00u;

constexpr unsigned XSTR = 16384 + 256;
constexpr unsigned BOX = 256 * XSTR;
constexpr unsigned XWSTR = 32768 + 256;
constexpr unsigned OFF_XW = 0;
constexpr unsigned OFF_WB = OFF_XW + 256 * XWSTR;
constexpr unsigned OFF_DH = OFF_WB + BOX;
constexpr unsigned LSTR = 32768 + 256;
constexpr unsigned PSZ = 32 * LSTR;
constexpr unsigned OFF_P = OFF_DH + BOX;
constexpr int REP = 8;
constexpr int GRP = 2;
constexpr int NRED = 32 * GRP;
constexpr unsigned RREP = GRP * 4096;
constexpr unsigned RSZ = REP * RREP;
constexpr unsigned OFF_R = OFF_P + 3 * PSZ;
constexpr unsigned OFF_SIG = OFF_R + 3 * RSZ;
constexpr unsigned OFF_FL = OFF_SIG + 8192;
constexpr unsigned OFF_TMO = OFF_FL + 2 * REP * 4096;
constexpr unsigned SPAN = (OFF_TMO + 256 + 4095) & ~4095u;
constexpr unsigned WS_HEAD = 4096;

constexpr int L_IMG = 0;
constexpr int L_STG = 16640;
constexpr int L_IN2 = L_STG + 16640;
constexpr int L_EN = L_IN2 + 16640;
constexpr int L_INB = L_EN + 16384;
constexpr int L_SG = L_INB + 24576;
constexpr int L_C3 = L_SG + 8192;
constexpr int L_CT = L_INB;
constexpr int L_XR = L_CT + 55296;
constexpr int L_MISC = L_XR + 16384;
constexpr int LDS_BYTES = L_MISC + 256;
constexpr int M_MX = L_MISC, M_GM = L_MISC + 8, M_CNT = L_MISC + 16, M_SIG = L_MISC + 64, M_NY = L_MISC + 128;

constexpr unsigned D_FLAG = 1000, D_WB = 1700, D_H1 = 900, D_H2R = 600, D_H2N = 3200, D_DH = 1300;
constexpr unsigned CODE_PF = 24576;

#define MFMA16(a, b, c) __builtin_amdgcn_mfma_f32_16x16x32_f16((a), (b), (c), 0, 0, 0)
#define FENCE() do { asm volatile("" ::: "memory"); __builtin_amdgcn_sched_barrier(0); } while (0)
#define PIN(x) asm volatile("" : "+v"(x))

DI void wave_sync() {
  __builtin_amdgcn_fence(__ATOMIC_RELEASE, "wavefront"); __builtin_amdgcn_wave_barrier(); __builtin_amdgcn_fence(__ATOMIC_ACQUIRE, "wavefront");
}
DI void wg_sync() {
  __builtin_amdgcn_fence(__ATOMIC_RELEASE, "workgroup"); __builtin_amdgcn_s_barrier(); __builtin_amdgcn_fence(__ATOMIC_ACQUIRE, "workgroup");
}
DI bool any_lane(bool p) { return __builtin_amdgcn_ballot_w64(p) != 0ull; }
DI unsigned umax(unsigned a, unsigned b) { return a > b ? a : b; }
DI float bf_lo(unsigned v) { return __builtin_bit_cast(float, v << 16); }
DI float bf_hi(unsigned v) { return __builtin_bit_cast(float, v & 0xFFFF0000u); }
DI unsigned pkh(float a, float b) { f2 t = {a, b}; return __builtin_bit_cast(unsigned, __builtin_convertvector(t, h2)); }
typedef __bf16 b2 __attribute__((ext_vector_type(2)));
DI unsigned pkbf(float a, float b) { f2 t = {a, b}; return __builtin_bit_cast(unsigned, __builtin_convertvector(t, b2)); }
DI unsigned nosent(unsigned v) { return v < 0xFFFFFFFEu ? v : 0xFFFFFFFEu; }
DI v4u nosent4(v4u v) { return v4u{nosent(v.x), nosent(v.y), nosent(v.z), nosent(v.w)}; }
DI float rsin(float rev) { return __builtin_amdgcn_sinf(rev); }
DI float rcos(float rev) { return __builtin_amdgcn_cosf(rev); }
template <int CTRL, int RM> DI float dpp_add(float v) {
  int o = __builtin_amdgcn_update_dpp(0, __builtin_bit_cast(int, v), CTRL, RM, 0xf, true);
  return v + __builtin_bit_cast(float, o);
}
DI float wave_total(float v) {
  v = dpp_add<0x111, 0xf>(v); v = dpp_add<0x112, 0xf>(v); v = dpp_add<0x114, 0xf>(v); v = dpp_add<0x118, 0xf>(v);
  v = dpp_add<0x142, 0xa>(v); v = dpp_add<0x143, 0xc>(v);
  return __builtin_bit_cast(float, __builtin_amdgcn_readlane(__builtin_bit_cast(int, v), 63));
}
template <int CTRL, int RM> DI unsigned dpp_maxu(unsigned v) {
  unsigned o = (unsigned)__builtin_amdgcn_update_dpp(0, (int)v, CTRL, RM, 0xf, true);
  return v > o ? v : o;
}
DI unsigned wave_maxu(unsigned v) {
  v = dpp_maxu<0x111, 0xf>(v); v = dpp_maxu<0x112, 0xf>(v); v = dpp_maxu<0x114, 0xf>(v); v = dpp_maxu<0x118, 0xf>(v);
  v = dpp_maxu<0x142, 0xa>(v); v = dpp_maxu<0x143, 0xc>(v);
  return (unsigned)__builtin_amdgcn_readlane((int)v, 63);
}
DI h8 neg8(h8 v) { v4u p = __builtin_bit_cast(v4u, v); p ^= 0x80008000u; return __builtin_bit_cast(h8, p); }
DI h4 tr_read(char* smem, int off) { return __builtin_bit_cast(h4, __builtin_amdgcn_ds_read_tr16_b64_v4i16((LAS s4*)(smem + off))); }
DI h8 cat(h4 a, h4 b) { return __builtin_shufflevector(a, b, 0, 1, 2, 3, 4, 5, 6, 7); }
DI f16v zero16() { f16v z; for (int i = 0; i < 16; ++i) z[i] = 0.f; return z; }

typedef __amdgpu_buffer_rsrc_t rsrc_t;
DI v4u ld_sc(rsrc_t rs, unsigned off) { return __builtin_amdgcn_raw_buffer_load_b128(rs, off, 0, SC1); }
DI void st_sc(rsrc_t rs, unsigned off, v4u v) { __builtin_amdgcn_raw_buffer_store_b128(v, rs, off, 0, SC1); }
DI bool give_up(rsrc_t rs, unsigned spins, unsigned tg) {
  __builtin_amdgcn_s_sleep(1);
  if (spins > SPINCAP) { __builtin_amdgcn_raw_buffer_store_b32(tg, rs, OFF_TMO, 0, SC1); return true; }
  return (spins & 1023u) == 1023u && (unsigned)__builtin_amdgcn_readfirstlane((int)__builtin_amdgcn_raw_buffer_load_b32(rs, OFF_TMO, 0, SC1)) == tg;
}
template <int N> DI void poll(rsrc_t rs, unsigned off, unsigned sbase, unsigned stride, unsigned tg, v4u (&x)[N]) {
#pragma nounroll
  for (unsigned spins = 0;; ++spins) {
    asm volatile("" ::: "memory");
#pragma unroll
    for (int m = 0; m < N; ++m) x[m] = __builtin_amdgcn_raw_buffer_load_b128(rs, off, sbase + m * stride, SC1);
    unsigned mx = 0u;
#pragma unroll
    for (int m = 0; m < N; ++m) mx = umax(umax(mx, umax(x[m].x, x[m].y)), umax(x[m].z, x[m].w));
    if (!any_lane(mx == SENT)) break;
    if (give_up(rs, spins, tg)) break;
  }
}
DI int kq(int g, int j) { return 16 * (j >> 2) + 4 * g + (j & 3); }
DI v4u rec_read(const char* src, int i, int sub) { return *(const v4u*)(src + i * 64 + ((sub ^ ((i >> 2) & 3)) << 4)); }
DI unsigned zip_lo(unsigned a, unsigned b) { return __builtin_amdgcn_perm(b, a, 0x05040100u); }
DI unsigned zip_hi(unsigned a, unsigned b) { return __builtin_amdgcn_perm(b, a, 0x07060302u); }
DI float h_lo(unsigned v) { return (float)__builtin_bit_cast(h2, v)[0]; }
DI float h_hi(unsigned v) { return (float)__builtin_bit_cast(h2, v)[1]; }
DI h2 ph(float a, float b) { f2 t = {a, b}; return __builtin_convertvector(t, h2); }
template <int SG> DI void cmul_h(const v4f& ar, const v4f& ai, const h2 (&c)[2], const h2 (&s)[2], v2u& ore, v2u& oim) {
#pragma unroll
  for (int k = 0; k < 2; ++k) {
    const h2 a = ph(ar[2 * k], ar[2 * k + 1]), b = ph(ai[2 * k], ai[2 * k + 1]);
    const h2 re = SG > 0 ? __builtin_elementwise_fma(-b, s[k], a * c[k]) : __builtin_elementwise_fma(b, s[k], a * c[k]);
    const h2 im = SG > 0 ? __builtin_elementwise_fma(a, s[k], b * c[k]) : __builtin_elementwise_fma(-a, s[k], b * c[k]);
    ore[k] = __builtin_bit_cast(unsigned, re); oim[k] = __builtin_bit_cast(unsigned, im);
  }
}
DI h8 join(const v2u& a, const v2u& b) { return __builtin_bit_cast(h8, v4u{a[0], a[1], b[0], b[1]}); }
DI unsigned clampE(unsigned mx) { unsigned E = mx >> 7; return E < 4u ? 4u : (E > 248u ? 248u : E); }
DI void stage_filter(char* dst, int w, int lane, const v4u& re, const v4u& im) {
  unsigned* stg = (unsigned*)dst + w * 516 + lane * 8;
  *(v4u*)stg = v4u{zip_lo(re[0], im[0]), zip_hi(re[0], im[0]), zip_lo(re[1], im[1]), zip_hi(re[1], im[1])};
  *(v4u*)(stg + 4) = v4u{zip_lo(re[2], im[2]), zip_hi(re[2], im[2]), zip_lo(re[3], im[3]), zip_hi(re[3], im[3])};
}
DI unsigned long long now() { return __builtin_amdgcn_s_memrealtime() * 24ull; }
DI void pace(unsigned long long t0, unsigned clks) {
#pragma nounroll
  while (now() - t0 < clks) __builtin_amdgcn_s_sleep(1);
}
DI v4u ld_filter(const u16* a, unsigned off) {
  return __builtin_amdgcn_raw_buffer_load_b128(__builtin_amdgcn_make_buffer_rsrc((void*)a, 0, 2048 * 513 * 2, 0x00020000), off, 0, 2);
}
DI bool tag_missing(const v4u& f, const v4u& tag) { return any_lane((f.x != tag.x) | (f.y != tag.y) | (f.z != tag.z)); }
typedef LAS unsigned* lds_u;
DI unsigned lds_add(char* smem, int off, unsigned v) { return __hip_atomic_fetch_add((lds_u)(smem + off), v, __ATOMIC_RELAXED, __HIP_MEMORY_SCOPE_WORKGROUP); }
DI void lds_max(char* smem, int off, unsigned v) { (void)__hip_atomic_fetch_max((lds_u)(smem + off), v, __ATOMIC_RELAXED, __HIP_MEMORY_SCOPE_WORKGROUP); }
DI unsigned mag_max(const v4u& re, const v4u& im) {
  unsigned mx = 0;
#pragma unroll
  for (int e = 0; e < 4; ++e) mx = umax(mx, umax(umax(re[e] & 0x7FFFu, (re[e] >> 16) & 0x7FFFu), umax(im[e] & 0x7FFFu, (im[e] >> 16) & 0x7FFFu)));
  return wave_maxu(mx);
}
DI float read_filter(const char* box, unsigned E, int c, int qd, int lane, h2 (&wc)[2][2], h2 (&wsn)[2][2]) {
  const float sc = __builtin_bit_cast(float, (251u - E) << 23);
#pragma unroll
  for (int T = 0; T < 2; ++T) {
    const v4u v = rec_read(box, qd * 64 + lane, 2 * c + T);
#pragma unroll
    for (int k = 0; k < 2; ++k) { wc[T][k] = ph(bf_lo(v[2 * k]) * sc, bf_lo(v[2 * k + 1]) * sc); wsn[T][k] = ph(bf_hi(v[2 * k]) * sc, bf_hi(v[2 * k + 1]) * sc); }
  }
  return __builtin_bit_cast(float, (E - 2u) << 23);
}

NOPK __global__ __launch_bounds__(512, 2) void gf_kernel(const u16* x, const u16* awr, const u16* awi, const u16* bwr, const u16* bwi, u16* out,
                                                    unsigned char* ws) {
  extern __shared__ __attribute__((aligned(16))) char smem[];
  unsigned cpf;
  {
    const rsrc_t rc = __builtin_amdgcn_make_buffer_rsrc((void*)(__builtin_amdgcn_s_getpc() & ~127ull), 0, CODE_PF, 0x00020000);
    cpf = __builtin_amdgcn_raw_buffer_load_b32(rc, threadIdx.x < CODE_PF / 512 ? (threadIdx.x * 4 + ((blockIdx.x >> 3) & 3)) * 128 : OOB, 0, 0);
  }
  FENCE();
  const int tid = threadIdx.x, lane = tid & 63, w = __builtin_amdgcn_readfirstlane(tid >> 6);
  const int l31 = lane & 31, h = lane >> 5, l15 = lane & 15, g = lane >> 4, b = blockIdx.x;
  const int c = w >> 2, qd = w & 3;
  const rsrc_t re = __builtin_amdgcn_make_buffer_rsrc(ws, 0, WS_HEAD, 0x00020000);
  const v4u sv = {SENT, SENT, SENT, SENT};
  const int mrow = 16 * (b >> 7) + 4 * ((b >> 4) & 3) + (w & 3);
  const int srow = 32 * (w < 4 ? mrow : (mrow ? 64 - mrow : 32)) + ((b >> 2) & 16) + (b & 15);
  const unsigned wofs = ((unsigned)(16 * (b >> 6) + (b & 15) + 64 * kq((b >> 4) & 3, w)) * 513u + 1u + 8u * lane) * 2u;
  v4u xr[2], wv[4];
  {
    const v4u* p = (const v4u*)(x + (size_t)srow * 1024 + l31 * 32 + h * 16);
    xr[0] = __builtin_nontemporal_load(p); xr[1] = __builtin_nontemporal_load(p + 1);
  }
  FENCE();
  const v2u ep = __builtin_amdgcn_raw_buffer_load_b64(re, 0, 0, SC1);
  FENCE();
  if (tid < 8) *(unsigned*)(smem + L_MISC + tid * 4) = 0u;
  {
    const int kind = w >> 1;
    const float step = kind == 0 ? (float)l31 * (-1.f / 32.f) : (kind == 1 ? (float)l15 * (1.f / 32.f) : (float)l31 * (1.f / 1024.f));
    const float base = step * (float)(16 * (w & 1) + 4 * h) + (kind == 1 ? (float)(l31 >> 4) * 0.25f : 0.f);
    const float amp = kind == 2 ? (1.f / 32.f) : 1.f;
    v4u cc, ss;
#pragma unroll
    for (int j = 0; j < 4; ++j) {
      const float a0 = base + step * (float)(8 * (j >> 1) + 2 * (j & 1)), a1 = a0 + step;
      cc[j] = pkh(rcos(a0) * amp, rcos(a1) * amp); ss[j] = pkh(rsin(a0) * amp, rsin(a1) * amp);
    }
    *(v4u*)(smem + L_EN + (2 * w) * 1024 + lane * 16) = cc; *(v4u*)(smem + L_EN + (2 * w + 1) * 1024 + lane * 16) = ss;
  }
  FENCE();
  wv[0] = ld_filter(awr, wofs); wv[1] = ld_filter(awi, wofs);
  FENCE();
  wg_sync();
  h8 c32[2], ns32[2], a1[2], a2[2];
  h2 twc[4][2], tws[4][2];
  {
    const char* cp = smem + L_EN + lane * 16;
#pragma unroll
    for (int st = 0; st < 2; ++st) {
      c32[st] = *(const h8*)(cp + (2 * st) * 1024); ns32[st] = *(const h8*)(cp + (2 * st + 1) * 1024);
      a1[st] = *(const h8*)(cp + (4 + 2 * st) * 1024); a2[st] = *(const h8*)(cp + (5 + 2 * st) * 1024);
      const v4u tc = *(const v4u*)(cp + (8 + 2 * st) * 1024), ts = *(const v4u*)(cp + (9 + 2 * st) * 1024);
#pragma unroll
      for (int e = 0; e < 4; ++e) { unsigned a = tc[e], bb = ts[e]; PIN(a); PIN(bb); twc[2 * st + (e >> 1)][e & 1] = __builtin_bit_cast(h2, a); tws[2 * st + (e >> 1)][e & 1] = __builtin_bit_cast(h2, bb); }
      PIN(c32[st]); PIN(ns32[st]); PIN(a1[st]); PIN(a2[st]);
    }
  }
  FENCE();
  {
    float v[16];
#pragma unroll
    for (int k = 0; k < 8; ++k) { const unsigned d = xr[k >> 2][k & 3]; v[2 * k] = bf_lo(d); v[2 * k + 1] = bf_hi(d); }
    *(v4u*)(smem + L_XR + tid * 16) = xr[0]; *(v4u*)(smem + L_XR + 8192 + tid * 16) = xr[1];
    const float piv = __builtin_bit_cast(float, __builtin_amdgcn_readfirstlane(__builtin_bit_cast(int, v[0])));
    float se = 0.f, so = 0.f, q2 = 0.f, q3 = 0.f;
#pragma unroll
    for (int k = 0; k < 8; ++k) {
      v[2 * k] -= piv; v[2 * k + 1] -= piv;
      se += v[2 * k]; so += v[2 * k + 1]; q2 += v[2 * k] * v[2 * k]; q3 += v[2 * k + 1] * v[2 * k + 1];
    }
    se = wave_total(se); so = wave_total(so); q2 = wave_total(q2 + q3);
    const float m = (se + so) * (1.f / 1024.f);
    q2 = __builtin_fmaxf(q2 * (1.f / 1024.f) - m * m, 0.f) + EPS;
    const float r0 = __builtin_amdgcn_rsqf(q2), mr = -m * r0;
    const float nyq = (se - so) * r0 * (1.f / 32.f);
    v4u g0, g1;
#pragma unroll
    for (int k = 0; k < 4; ++k) { g0[k] = pkh(v[2 * k] * r0 + mr, v[2 * k + 1] * r0 + mr); g1[k] = pkh(v[8 + 2 * k] * r0 + mr, v[9 + 2 * k] * r0 + mr); }
    char* img = smem + L_IMG + w * 2048 + l31 * 64 + h * 32;
    *(v4u*)img = g0; *(v4u*)(img + 16) = g1;
    if (lane == 0) *(float*)(smem + M_SIG + w * 4) = q2 * r0;
    wave_sync();
    const int tb = L_IMG + w * 2048 + (4 * h + (l15 >> 2)) * 64 + (16 * (g & 1) + 4 * (l15 & 3)) * 2;
    f16v dr = zero16(), di = zero16();
#pragma unroll
    for (int st = 0; st < 2; ++st) {
      const h8 A = cat(tr_read(smem, tb + st * 1024), tr_read(smem, tb + st * 1024 + 512));
      dr = MFMA(A, c32[st], dr); di = MFMA(A, ns32[st], di);
    }
    v2u er[4], ei[4];
#pragma unroll
    for (int k = 0; k < 4; ++k)
      cmul_h<-1>(v4f{dr[4 * k], dr[4 * k + 1], dr[4 * k + 2], dr[4 * k + 3]}, v4f{di[4 * k], di[4 * k + 1], di[4 * k + 2], di[4 * k + 3]}, twc[k], tws[k], er[k], ei[k]);
    f16v d2 = zero16();
#pragma unroll
    for (int st = 0; st < 2; ++st) { d2 = MFMA(a1[st], join(er[2 * st], er[2 * st + 1]), d2); d2 = MFMA(a2[st], join(ei[2 * st], ei[2 * st + 1]), d2); }
    { const float d0 = d2[0], d8 = d2[8]; d2[0] = lane == 0 ? nyq : d0; d2[8] = lane == 0 ? 0.f : d8; }
    unsigned* stg = (unsigned*)(smem + L_STG) + w * 516 + l31 + 128 * h;
#pragma unroll
    for (int r = 0; r < 8; ++r) stg[32 * ((r & 3) + 8 * (r >> 2))] = pkh(d2[r], d2[r + 8]);
  }
  stage_filter(smem + L_IN2 + 32, w, lane, wv[0], wv[1]);
  wg_sync();
  asm volatile("" :: "v"(cpf));
  const unsigned long long tag64 = (((unsigned long long)ep.y << 32) | ep.x) + 1ull;
  const v4u tag = {(unsigned)tag64, (unsigned)(tag64 >> 32), ~(unsigned)tag64, 0u};
  unsigned long long tf = 0;
  const rsrc_t rs = __builtin_amdgcn_make_buffer_rsrc(ws + WS_HEAD + ((unsigned)__builtin_amdgcn_readfirstlane((int)tag.x) & 1u) * SPAN, 0, SPAN, 0x00020000);
#pragma unroll
  for (int m = 0; m < 4; ++m) {
    const int p = m * 512 + tid, isw = (p >> 2) & 1, kap = (p >> 3) * 2 + ((p >> 1) & 1);
    const unsigned* sp = (const unsigned*)(smem + (isw ? L_IN2 + 32 : L_STG)) + (p & 1) * (4 * 516) + ((kap - isw) & 511);
    const v4u v = {sp[0], sp[516], sp[1032], sp[1548]};
    st_sc(rs, OFF_XW + (p >> 3) * XWSTR + b * 128 + (p & 7) * 16, v);
  }
  if (w < 4) st_sc(rs, OFF_P + (tid >> 3) * LSTR + b * 128 + (tid & 7) * 16, sv);
  else if (w == 4 && b < NRED) st_sc(rs, OFF_R + (lane >> 3) * RREP + (b % GRP) * 4096 + (b / GRP) * 128 + (lane & 7) * 16, sv);
  else if (w == 5) __builtin_amdgcn_raw_buffer_store_b32(*(const unsigned*)(smem + M_SIG + (lane & 7) * 4), rs, lane < 8 ? OFF_SIG + b * 32 + lane * 4 : OOB, 0, SC1);
  FENCE();
  { const unsigned mx = mag_max(wv[0], wv[1]); if (lane == 0) lds_max(smem, M_MX, mx); }
  FENCE();
#pragma unroll
  for (int it = 0; it < 3; ++it) {
    if (it == 2) {
      asm volatile("s_waitcnt vmcnt(0)" ::: "memory");
      wg_sync();
      if (tid < REP) { v4u fl = tag; fl.w = *(const unsigned*)(smem + M_MX); st_sc(rs, OFF_FL + tid * 4096 + b * 16, fl); }
      tf = now();
      wv[2] = ld_filter(bwr, wofs); wv[3] = ld_filter(bwi, wofs);
      FENCE();
    }
    const int m = 3 * c + it;
    float step, base = 0.f;
    if (m == 0) step = (float)(16 * qd + l15) * (1.f / 64.f);
    else if (m < 3) step = (float)(4 * (16 * (m - 1) + l15) + qd) * (1.f / 128.f);
    else if (m < 5) { step = (float)(16 * (m - 3) + l15) * (1.f / 32.f); base = step * (float)qd * 0.25f; }
    else { step = (float)(16 * (qd >> 1) + l15) * (1.f / 64.f); base = step * (float)(32 * (qd & 1)); }
    base += step * (float)(4 * g);
    base -= __builtin_floorf(base);
    h8 cc, ss;
#pragma unroll
    for (int j = 0; j < 8; ++j) {
      const float an = base + step * (float)(16 * (j >> 2) + (j & 3));
      float sn = rsin(an);
      if (j == 0 && m == 0 && g == 0) sn = (l15 & 1) ? -1.f : 1.f;
      if (m == 5 && qd < 2 && l15 == 0) sn = (j & 1) ? -1.f : 1.f;
      cc[j] = (_Float16)rcos(an); ss[j] = (_Float16)sn;
    }
    char* cp = smem + L_CT + (qd * 12 + m * 2) * 1024 + lane * 16;
    *(h8*)cp = cc; *(h8*)(cp + 1024) = ss;
  }
  if (w < 3) {
    v4f cc, ss;
    const float step = (float)(w == 2 ? 16 + l15 : l15) * (1.f / 2048.f), base = step * (float)(4 * g + (w == 1 ? 16 : 0));
#pragma unroll
    for (int r = 0; r < 4; ++r) { const float an = base + step * (float)r; cc[r] = rcos(an) * 0.125f; ss[r] = rsin(an) * 0.125f; }
    *(v4f*)(smem + L_CT + (48 + w) * 1024 + lane * 16) = cc; *(v4f*)(smem + L_CT + (51 + w) * 1024 + lane * 16) = ss;
  }
  wg_sync();
  h8 C1, S1, N1, CA[2], SA[2], NA[2], CB[2], SB[2], NB[2], C4[2], S4[2];
  h2 tch[3][2], tsh[3][2];
  {
    const char* cp = smem + L_CT + qd * 12288 + lane * 16;
    C1 = *(const h8*)cp; S1 = *(const h8*)(cp + 1024); N1 = neg8(S1);
    PIN(C1); PIN(S1); PIN(N1);
#pragma unroll
    for (int t = 0; t < 2; ++t) {
      CA[t] = *(const h8*)(cp + (2 + 2 * t) * 1024); SA[t] = *(const h8*)(cp + (3 + 2 * t) * 1024);
      CB[t] = *(const h8*)(cp + (6 + 2 * t) * 1024); SB[t] = *(const h8*)(cp + (7 + 2 * t) * 1024);
      const char* c4 = smem + L_CT + (2 * c + t) * 12288 + 10240 + lane * 16;
      C4[t] = *(const h8*)c4; S4[t] = *(const h8*)(c4 + 1024);
      NA[t] = neg8(SA[t]); NB[t] = neg8(SB[t]);
      PIN(CA[t]); PIN(SA[t]); PIN(CB[t]); PIN(SB[t]); PIN(NA[t]); PIN(NB[t]); PIN(C4[t]); PIN(S4[t]);
    }
#pragma unroll
    for (int t = 0; t < 3; ++t) {
      const v4f tc = *(const v4f*)(smem + L_CT + (48 + t) * 1024 + lane * 16), ts = *(const v4f*)(smem + L_CT + (51 + t) * 1024 + lane * 16);
#pragma unroll
      for (int k = 0; k < 2; ++k) { tch[t][k] = ph(tc[2 * k], tc[2 * k + 1]); tsh[t][k] = ph(ts[2 * k], ts[2 * k + 1]); PIN(tch[t][k]); PIN(tsh[t][k]); }
    }
  }
  FENCE();
  if (tid >= 256) {
    pace(tf, D_FLAG);
    v4u f;
#pragma nounroll
    for (unsigned spins = 0;; ++spins) {
      asm volatile("" ::: "memory");
      f = ld_sc(rs, OFF_FL + (b & 7) * 4096 + (tid - 256) * 16);
      if (!tag_missing(f, tag)) break;
      if (give_up(rs, spins, tag.x)) break;
    }
    const unsigned mx = wave_maxu(f.w);
    if (lane == 0) lds_max(smem, M_GM, mx);
  }
  wg_sync();
  {
    v4u xh[4];
#pragma unroll
    for (int m = 0; m < 4; ++m) xh[m] = ld_sc(rs, OFF_XW + b * XWSTR + m * 8192 + tid * 16);
    const v4u sgx = ld_sc(rs, OFF_SIG + tid * 16);
    FENCE();
    if (b == 0 && tid == 0) __builtin_amdgcn_raw_buffer_store_b64(v2u{tag.x, tag.y}, re, 0, 0, SC1);
    FENCE();
    stage_filter(smem + L_STG, w, lane, wv[2], wv[3]);
    const unsigned mx = mag_max(wv[2], wv[3]);
    if (lane == 0) lds_max(smem, M_MX + 4, mx);
    FENCE();
#pragma unroll
    for (int m = 0; m < 4; ++m) {
      const int i = m * 64 + (tid >> 3);
      *(v4u*)(smem + ((tid & 4) ? L_EN : L_INB) + i * 64 + (((tid & 3) ^ ((i >> 2) & 3)) << 4)) = xh[m];
    }
    *(v4u*)(smem + L_SG + tid * 16) = sgx;
  }
  wg_sync();
  h2 wc[2][2][2], wsn[2][2][2];
  float wun[2], gscale = 1.f;
  const int t4 = tid & 255;
  const int nh = w >> 2, Ts = (w >> 1) & 1, cl = l15 >> 3, slane = g * 16 + (w & 1) * 8 + (l15 & 7), rec = nh * 128 + Ts * 64 + slane;
  const float imk = (b == 0 && cl == 0) ? 0.f : 1.f;
  const bool sp0 = (nh == 0) && (g == 0);
  v4f zr[2], zi[2];
  v4f ur[2], ui[2];
  const v4f zero4 = {0.f, 0.f, 0.f, 0.f};
#pragma unroll
  for (int k = 0; k < 2; ++k) {
    const v4u d = rec_read(smem + L_INB, rec, 2 * cl + k);
    const v4f sg0 = *(const v4f*)(smem + L_SG + rec * 32 + k * 16);
#pragma unroll
    for (int r = 0; r < 4; ++r) { ur[k][r] = h_lo(d[r]); ui[k][r] = h_hi(d[r]); }
    zr[k] = ur[k] * sg0; zi[k] = ui[k] * sg0;
    PIN(zr[k]); PIN(zi[k]);
  }

#pragma unroll
  for (int layer = 0; layer < NLAYER; ++layer) {
    h8 F[2][4];
    {
      v4f er = ur[0] + ur[1], ei = ui[0] + ui[1], orr = ur[0] - ur[1], oi = ui[0] - ui[1];
      { const float a0 = ur[0][0], a1 = ui[0][0], b0 = ur[1][0], b1 = ui[1][0];
        er[0] = sp0 ? a0 : a0 + b0; ei[0] = sp0 ? a1 : a1 + b1; oi[0] = sp0 ? b0 : a1 - b1; orr[0] = sp0 ? -b1 : a0 - b0; }
      char* ex = smem + L_IMG + c * 8320 + lane * 16;
      char* eo = smem + L_IMG + cl * 8320 + slane * 16 + Ts * 4096 + nh * 8;
      *(v2u*)(eo) = v2u{pkh(er[0], er[1]), pkh(er[2], er[3])};
      *(v2u*)(eo + 1024) = v2u{pkh(ei[0], ei[1]), pkh(ei[2], ei[3])};
      *(v2u*)(eo + 2048) = v2u{pkh(orr[0], orr[1]), pkh(orr[2], orr[3])};
      *(v2u*)(eo + 3072) = v2u{pkh(oi[0], oi[1]), pkh(oi[2], oi[3])};
      wg_sync();
#pragma unroll
      for (int T = 0; T < 2; ++T)
#pragma unroll
        for (int k = 0; k < 4; ++k) F[T][k] = *(const h8*)(ex + T * 4096 + k * 1024);
    }
    if (layer == 0) {
      wun[0] = read_filter(smem + L_EN, clampE(*(const unsigned*)(smem + M_GM)), c, qd, lane, wc[0], wsn[0]);
    }
    if (layer == 1) {
      const unsigned EB = clampE(*(const unsigned*)(smem + M_GM + 4));
      wun[1] = read_filter(smem + L_STG, EB, c, qd, lane, wc[1], wsn[1]);
      gscale = __builtin_bit_cast(float, (umax(EB, clampE(*(const unsigned*)(smem + M_GM))) + 1u) << 23);
      if (tid < 256) st_sc(rs, OFF_P + 2 * PSZ + (tid >> 3) * LSTR + b * 128 + (tid & 7) * 16, sv);
      else if (b < NRED && tid < 320) st_sc(rs, OFF_R + 2 * RSZ + ((tid - 256) >> 3) * RREP + (b % GRP) * 4096 + (b / GRP) * 128 + (tid & 7) * 16, sv);
    }
    if (layer == 2) {
      st_sc(rs, OFF_DH + b * XSTR + tid * 16, sv); st_sc(rs, OFF_DH + b * XSTR + 8192 + tid * 16, sv);
    }
    v4f ar[2], ai[2];
    v2u hr[2], hi[2];
#pragma unroll
    for (int T = 0; T < 2; ++T) {
      ar[T] = MFMA16(F[T][0], C1, zero4); ai[T] = MFMA16(F[T][1], C1, zero4);
      ar[T] = MFMA16(F[T][3], S1, ar[T]); ai[T] = MFMA16(F[T][2], N1, ai[T]);
      cmul_h<-1>(ar[T], ai[T], tch[T], tsh[T], hr[T], hi[T]);
    }
    {
      const h8 fr = join(hr[0], hr[1]), fi = join(hi[0], hi[1]);
#pragma unroll
      for (int T = 0; T < 2; ++T) {
        ar[T] = MFMA16(CA[T], fr, zero4); ai[T] = MFMA16(CA[T], fi, zero4);
        ar[T] = MFMA16(SA[T], fi, ar[T]); ai[T] = MFMA16(NA[T], fr, ai[T]);
      }
    }
#pragma unroll
    for (int T = 0; T < 2; ++T) cmul_h<1>(ar[T], ai[T], wc[layer & 1][T], wsn[layer & 1][T], hr[T], hi[T]);
    if (layer == 0) {
#pragma unroll
      for (int m = 0; m < 2; ++m) {
        const int p = m * 512 + tid;
        const unsigned* sp = (const unsigned*)(smem + L_STG) + (p & 1) * (4 * 516) + (((p >> 1) - 1) & 511);
        st_sc(rs, OFF_WB + b * 64 + (p >> 2) * XSTR + (p & 3) * 16, v4u{sp[0], sp[516], sp[1032], sp[1548]});
      }
      if (w < 4) st_sc(rs, OFF_P + PSZ + (tid >> 3) * LSTR + b * 128 + (tid & 7) * 16, sv);
      else if (w == 4 && b < NRED) st_sc(rs, OFF_R + RSZ + (lane >> 3) * RREP + (b % GRP) * 4096 + (b / GRP) * 128 + (lane & 7) * 16, sv);
    }
    h8 V[4];
    {
      const h8 fr = join(hr[0], hr[1]), fi = join(hi[0], hi[1]);
      char* ex = smem + L_IN2 + c * 8320 + lane * 16;
#pragma unroll
      for (int T = 0; T < 2; ++T) {
        ar[T] = MFMA16(fr, CB[T], zero4); ai[T] = MFMA16(fi, CB[T], zero4);
        ar[T] = MFMA16(fi, NB[T], ar[T]); ai[T] = MFMA16(fr, SB[T], ai[T]);
        cmul_h<1>(ar[T], ai[T], tch[2 * T], tsh[2 * T], hr[T], hi[T]);
        *(v2u*)(ex + T * 4096 + (qd >> 1) * 1024 + (qd & 1) * 8) = hr[T];
        *(v2u*)(ex + T * 4096 + 2048 + (qd >> 1) * 1024 + (qd & 1) * 8) = hi[T];
      }
      wg_sync();
#pragma unroll
      for (int k = 0; k < 4; ++k) V[k] = *(const h8*)(smem + L_IN2 + cl * 8320 + slane * 16 + Ts * 4096 + k * 1024);
    }
    {
      v4f P = MFMA16(C4[0], V[0], zero4), R = MFMA16(C4[0], V[2], zero4), Q = MFMA16(S4[0], V[2], zero4), Tt = MFMA16(S4[0], V[0], zero4);
      P = MFMA16(C4[1], V[1], P); R = MFMA16(C4[1], V[3], R); Q = MFMA16(S4[1], V[3], Q); Tt = MFMA16(S4[1], V[1], Tt);
      v4f yr0 = P - Q, yi0 = R + Tt, yr1 = P + Q, yi1 = R - Tt;
      { const float p0 = P[0], q0 = Q[0], r0 = R[0], t0 = Tt[0];
        yr0[0] = sp0 ? p0 : p0 - q0; yi0[0] = sp0 ? r0 : r0 + t0; yr1[0] = sp0 ? t0 : p0 + q0; yi1[0] = sp0 ? q0 : r0 - t0; }
      const float us = wun[layer & 1], vs = us * imk;
      zr[0] += yr0 * us; zi[0] += yi0 * vs; zr[1] += yr1 * us; zi[1] += yi1 * vs;
    }
    if (layer < NLAYER - 1) {
      v4f m0 = zr[0] * zr[0] + zi[0] * zi[0], m1 = zr[1] * zr[1] + zi[1] * zi[1];
      if (b == 0) { const float hw = 0.5f + 0.5f * imk; m0 *= hw; m1 *= hw; }
      const unsigned pb = OFF_P + layer * PSZ, rb = OFF_R + layer * RSZ;
#pragma unroll
      for (int k = 0; k < 4; ++k) { m0[k] = dpp_add<0x128, 0xf>(m0[k]); m1[k] = dpp_add<0x128, 0xf>(m1[k]); }
      asm volatile("s_waitcnt vmcnt(0)" ::: "memory");
      st_sc(rs, cl ? OOB : pb + b * 128 + (rec >> 3) * LSTR + (rec & 7) * 16,
            nosent4(v4u{pkbf(m0[0], m0[1]), pkbf(m0[2], m0[3]), pkbf(m1[0], m1[1]), pkbf(m1[2], m1[3])}));
      unsigned long long ta = now();
      v4f dr[2], di[2];
      if (layer == 0) {
        unsigned arr = 0;
        if (lane == 0) arr = lds_add(smem, M_CNT, 1u);
        if (__builtin_amdgcn_readfirstlane((int)arr) == 7) {
          v4u fl = tag; fl.w = *(const unsigned*)(smem + M_MX + 4);
          if (lane < REP) st_sc(rs, OFF_FL + (REP + lane) * 4096 + b * 16, fl);
        }
        if (c == 1) {
          const unsigned fo = (qd * 16 + l15 + 64 * g) * 16;
          pace(ta, D_WB);
          v4u f;
#pragma nounroll
          for (unsigned spins = 0;; ++spins) {
            asm volatile("" ::: "memory");
            f = __builtin_amdgcn_raw_buffer_load_b128(rs, fo, OFF_FL + (REP + (b & 7)) * 4096, SC1);
            if (!tag_missing(f, tag)) break;
            if (give_up(rs, spins, tag.x)) break;
          }
          asm volatile("" ::: "memory");
          v4u x4[4];
#pragma unroll
          for (int m = 0; m < 4; ++m) x4[m] = __builtin_amdgcn_raw_buffer_load_b128(rs, t4 * 16, OFF_WB + b * XSTR + m * 4096, SC1);
          const unsigned mx = wave_maxu(f.w);
          if (lane == 0) lds_max(smem, M_GM + 4, mx);
#pragma unroll
          for (int m = 0; m < 4; ++m) { const int i = m * 64 + (t4 >> 2); *(v4u*)(smem + L_STG + i * 64 + (((t4 & 3) ^ ((i >> 2) & 3)) << 4)) = x4[m]; }
        }
      }
      if (b < NRED && w == 0) {
        v4u sel;
#pragma unroll
        for (int k = 0; k < 4; ++k) sel[k] = ((l15 & 7) == 2 * k ? 0x3F80u : 0u) | ((l15 & 7) == 2 * k + 1 ? 0x3F800000u : 0u);
        const unsigned ps = pb + (b / GRP) * LSTR + (b % GRP) * (32768 / GRP);
        unsigned ro = ((l15 >> 3) + (g >> 1) * 2) * RREP + (l15 & 7) * 16 + (g & 1) * 8, lo = lane * 16;
        PIN(sel); PIN(ro); PIN(lo);
        pace(ta, D_H1);
        v4f acc;
#pragma nounroll
        for (unsigned spins = 0;; ++spins) {
          asm volatile("" ::: "memory");
          v4u xx[32 / GRP];
#pragma unroll
          for (int m = 0; m < 32 / GRP; ++m) xx[m] = __builtin_amdgcn_raw_buffer_load_b128(rs, lo + (m & 3) * 1024, ps + (m >> 2) * 4096, SC1);
          unsigned mx = 0u;
          acc = zero4;
#pragma unroll
          for (int m = 0; m < 32 / GRP; ++m) {
            mx = umax(umax(mx, umax(xx[m].x, xx[m].y)), umax(xx[m].z, xx[m].w));
            acc = __builtin_amdgcn_mfma_f32_16x16x32_bf16(__builtin_bit_cast(b8, sel), __builtin_bit_cast(b8, xx[m]), acc, 0, 0, 0);
          }
          if (!any_lane(mx == SENT)) break;
          if (give_up(rs, spins, tag.x)) break;
        }
#pragma unroll
        for (int k = 0; k < 4; ++k) acc[k] = dpp_add<0x128, 0xf>(acc[k]);
        const v2u o = {nosent(pkbf(acc[0], acc[1])), nosent(pkbf(acc[2], acc[3]))};
        const unsigned rsb = rb + (b % GRP) * 4096 + (b / GRP) * 128;
        __builtin_amdgcn_raw_buffer_store_b64(o, rs, ro, rsb, SC1);
        __builtin_amdgcn_raw_buffer_store_b64(o, rs, ro, rsb + 4 * RREP, SC1);
        ta = now() - (D_H2N - D_H2R);
      }
      if (layer == 1) {
        const int n2 = 16 * ((l31 >> 2) & 1) + 4 * (l31 >> 3) + (l31 & 3);
        if (w < 3) {
          h8 cc, ss;
#pragma unroll
          for (int j = 0; j < 8; ++j) {
            const int kk = w == 0 ? 8 * h + j : 16 * (w - 1) + 8 * (j >> 2) + 4 * ((j >> 1) & 1) + 2 * h + (j & 1);
            const float an = (float)((kk * (w == 0 ? n2 : l31)) & 31) * (1.f / 32.f);
            cc[j] = (_Float16)rcos(an); ss[j] = (_Float16)rsin(w == 0 ? an : -an);
          }
          *(h8*)(smem + L_C3 + (2 * w) * 1024 + lane * 16) = cc; *(h8*)(smem + L_C3 + (2 * w + 1) * 1024 + lane * 16) = ss;
        } else if (w < 7) {
          v4f cc, ss;
#pragma unroll
          for (int j = 0; j < 4; ++j) {
            const int k1 = 8 * (w - 3) + 4 * (j >> 1) + 2 * h + (j & 1);
            const float an = (float)(k1 * n2) * (1.f / 1024.f);
            cc[j] = rcos(an) * 0.25f; ss[j] = rsin(an) * 0.25f;
          }
          *(v4f*)(smem + L_C3 + (2 * w) * 1024 + lane * 16) = cc; *(v4f*)(smem + L_C3 + (2 * w + 1) * 1024 + lane * 16) = ss;
        }
      }
      if (layer == NLAYER - 2) {
        const float ginv = __builtin_bit_cast(float, 0x7F000000u - __builtin_bit_cast(unsigned, gscale));
#pragma unroll
        for (int k = 0; k < 2; ++k) {
          const v4u d = rec_read(smem + L_INB, rec, 2 * cl + k);
          const v4f sg0 = *(const v4f*)(smem + L_SG + rec * 32 + k * 16);
          v4f z0r, z0i;
#pragma unroll
          for (int r = 0; r < 4; ++r) { z0r[r] = h_lo(d[r]) * sg0[r]; z0i[r] = h_hi(d[r]) * sg0[r]; }
          PIN(z0r); PIN(z0i);
          dr[k] = (zr[k] - z0r) * ginv; di[k] = (zi[k] - z0i) * ginv;
          PIN(dr[k]); PIN(di[k]);
        }
        wun[(NLAYER - 1) & 1] *= ginv;
      }
      {
        v2u y[GRP];
        pace(ta, D_H2N);
#pragma nounroll
        for (unsigned spins = 0;; ++spins) {
          asm volatile("" ::: "memory");
          unsigned mx = 0u;
#pragma unroll
          for (int k = 0; k < GRP; ++k) y[k] = __builtin_amdgcn_raw_buffer_load_b64(rs, rec * 16 + cl * 8, rb + (b & 7) * RREP + k * 4096, SC1);
#pragma unroll
          for (int k = 0; k < GRP; ++k) mx = umax(mx, umax(y[k].x, y[k].y));
          if (!any_lane(mx == SENT)) break;
          if (give_up(rs, spins, tag.x)) break;
        }
#pragma unroll
        for (int r = 0; r < 4; ++r) {
          float sum = 0.f;
#pragma unroll
          for (int k = 0; k < GRP; ++k) { const unsigned a = y[k][r >> 1]; sum += (r & 1) ? bf_hi(a) : bf_lo(a); }
          const int rr = __builtin_bit_cast(int, __builtin_amdgcn_rsqf(sum * (1.f / 512.f) + EPS));
          const float r0 = __builtin_bit_cast(float, __builtin_amdgcn_update_dpp(rr, rr, 0x128, 0xf, 0xc, false));
          const float r1 = __builtin_bit_cast(float, __builtin_amdgcn_update_dpp(rr, rr, 0x128, 0xf, 0x3, false));
          ur[0][r] = zr[0][r] * r0; ui[0][r] = zi[0][r] * r0; ur[1][r] = zr[1][r] * r1; ui[1][r] = zi[1][r] * r1;
        }
        if (layer == NLAYER - 2) { zr[0] = dr[0]; zr[1] = dr[1]; zi[0] = di[0]; zi[1] = di[1]; }
      }
    }
  }
  {
    const int key = ((b >> 2) & 1) | (((b >> 4) & 3) << 1);
    unsigned* stg = (unsigned*)(smem + L_IMG + w * 2560);
    unsigned* dst = stg + (g * 8 + (l15 & 7)) * 20 + cl;
#pragma unroll
    for (int q = 0; q < 8; ++q) dst[(q ^ key) * 2] = nosent(pkh(zr[q >> 2][q & 3], zi[q >> 2][q & 3]));
    wave_sync();
#pragma unroll
    for (int m = 0; m < 2; ++m) {
      const int rho = m * 16 + (lane >> 2);
      const v4u v = *(const v4u*)(stg + rho * 20 + (lane & 3) * 4);
      st_sc(rs, OFF_DH + b * 64 + (nh * 128 + Ts * 64 + (rho >> 3) * 16 + (w & 1) * 8 + (rho & 7)) * XSTR + (lane & 3) * 16, v);
    }
  }
  const unsigned long long td = now();
  {
    const char* c3 = smem + L_C3 + lane * 16;
    h8 Cq = *(const h8*)c3, Sq = *(const h8*)(c3 + 1024), Cz[2] = {*(const h8*)(c3 + 2048), *(const h8*)(c3 + 4096)}, Nz[2] = {*(const h8*)(c3 + 3072), *(const h8*)(c3 + 5120)};
    h2 uc[4][2], us[4][2];
#pragma unroll
    for (int k = 0; k < 4; ++k) {
      const v4f tc = *(const v4f*)(c3 + (6 + 2 * k) * 1024), ts = *(const v4f*)(c3 + (7 + 2 * k) * 1024);
#pragma unroll
      for (int e = 0; e < 2; ++e) { uc[k][e] = ph(tc[2 * e], tc[2 * e + 1]); us[k][e] = ph(ts[2 * e], ts[2 * e + 1]); PIN(uc[k][e]); PIN(us[k][e]); }
    }
    xr[0] = *(const v4u*)(smem + L_XR + tid * 16); xr[1] = *(const v4u*)(smem + L_XR + 8192 + tid * 16);
    float xf[16];
#pragma unroll
    for (int k = 0; k < 8; ++k) { const unsigned d = xr[k >> 2][k & 3]; xf[2 * k] = bf_lo(d); xf[2 * k + 1] = bf_hi(d); PIN(xf[2 * k]); PIN(xf[2 * k + 1]); }
    float nsg = h ? 0.f : ((l31 & 1) ? -0.5f : 0.5f);
    PIN(nsg);
    PIN(Cq); PIN(Sq); PIN(Cz[0]); PIN(Cz[1]); PIN(Nz[0]); PIN(Nz[1]);
    FENCE();
    {
      v4u y[2];
      pace(td, D_DH);
      poll<2>(rs, tid * 16, OFF_DH + b * XSTR, 8192, tag.x, y);
      if (tid < 4) { *(v2u*)(smem + M_NY + tid * 8) = v2u{y[0].x, y[0].z}; y[0].x = 0u; y[0].z = 0u; }
      *(v4u*)(smem + L_EN + tid * 16) = y[0]; *(v4u*)(smem + L_EN + 8192 + tid * 16) = y[1];
    }
    wg_sync();
    const int qq = l15 >> 2, pp = l15 & 3;
    const int tb = L_EN + (4 * (g & 1) + pp + 16 * (8 * h + qq)) * 64 + ((w ^ ((g & 1) | (qq << 1))) << 3);
    const float ny = h_lo(*(const unsigned*)(smem + M_NY + w * 4)) * nsg;
    const float osc = gscale * 0.25f;
    f16v acc = zero16();
#pragma unroll
    for (int T = 0; T < 2; ++T) {
      const h8 A = cat(tr_read(smem, tb + T * 512), tr_read(smem, tb + T * 512 + 4096));
      const f16v pa = MFMA(A, Cq, zero16()), pb = MFMA(A, Sq, zero16());
      v2u er[2], ei[2];
#pragma unroll
      for (int k = 0; k < 2; ++k) {
        v4f xr0, xi0;
#pragma unroll
        for (int j = 0; j < 4; ++j) { xr0[j] = pa[8 * k + 2 * j] - pb[8 * k + 2 * j + 1]; xi0[j] = pb[8 * k + 2 * j] + pa[8 * k + 2 * j + 1]; }
        if (T == 0 && k == 0) xr0[0] += ny;
        cmul_h<1>(xr0, xi0, uc[2 * T + k], us[2 * T + k], er[k], ei[k]);
      }
      acc = MFMA(join(er[0], er[1]), Cz[T], acc); acc = MFMA(join(ei[0], ei[1]), Nz[T], acc);
    }
    v4u ov[2];
#pragma unroll
    for (int k = 0; k < 8; ++k) ov[k >> 2][k & 3] = pkbf(acc[2 * k] * osc + xf[2 * k], acc[2 * k + 1] * osc + xf[2 * k + 1]);
    v4u* p = (v4u*)(out + (size_t)srow * 1024 + l31 * 32 + h * 16);
    p[0] = ov[0]; p[1] = ov[1];
  }
}

extern "C" void kernel_launch(void* const* d_in, const int* in_sizes, int n_in, void* d_out, int out_size, void* d_ws, size_t ws_size,
                              hipStream_t stream) {
  (void)hipFuncSetAttribute((const void*)gf_kernel, hipFuncAttributeMaxDynamicSharedMemorySize, LDS_BYTES);
  gf_kernel<<<256, 512, LDS_BYTES, stream>>>((const u16*)d_in[0], (const u16*)d_in[1], (const u16*)d_in[2], (const u16*)d_in[4],
                                             (const u16*)d_in[5], (u16*)d_out, (unsigned char*)d_ws);
}
```
